```python
import torch

def _softmax(x, axis=-1):
    return torch.softmax(x, dim=axis)
def _gelu(x):
    return torch.nn.functional.gelu(x, approximate="tanh")
def _silu(x):
    return torch.nn.functional.silu(x)
def _sigmoid(x):
    return torch.sigmoid(x)
def _layernorm(x, axis=-1, eps=1e-5):
    m = x.mean(dim=axis, keepdim=True)
    v = x.var(dim=axis, keepdim=True, unbiased=False)
    return (x - m) / torch.sqrt(v + eps)
def _scatter_add(tgt, idx, src, axis=0):
    return tgt.clone().index_add_(axis, idx, src)
def _segment_sum(src, seg, n):
    out = torch.zeros((n,) + src.shape[1:], dtype=src.dtype)
    return out.index_add_(0, seg, src)
def _pad(x, pw):
    flat = tuple(v for pair in reversed(pw) for v in pair)
    return torch.nn.functional.pad(x, flat)

kernel_name = 'gated_mix__linear_attn_block__dense_activation_mish__prompt'

def setup_inputs():

    _w = (lambda a: (lambda t: t.to(torch.bfloat16) if t.is_floating_point() else t)(torch.as_tensor(a)))
    def _A_setup():
        B, S, D = 1, 2048, 1024
        return {
            "x": _w((lambda *s: torch.randn(*s, dtype=torch.bfloat16) * 0.05)(B, S, D)),
            "wqkv": _w((lambda *s: torch.randn(*s, dtype=torch.bfloat16) * 0.8)(D, 3 * D)),
            "wo": _w((lambda *s: torch.randn(*s, dtype=torch.bfloat16) * 0.05)(D, D)),
        }
    _A_in = _A_setup()

    def _B_setup():
        B, S, D, F = 1, 2048, 1024, 3584
        return {
            "x": _w((lambda *s: torch.randn(*s, dtype=torch.bfloat16) * 0.05)(B, S, D)),
            "w1": _w((lambda *s: torch.randn(*s, dtype=torch.bfloat16) * 12.8)(D, F) * (D ** -0.5)),
            "b1": _w((lambda *s: torch.randn(*s, dtype=torch.bfloat16) * 0.4)(F)),
            "w2": _w((lambda *s: torch.randn(*s, dtype=torch.bfloat16) * 0.05)(F, D) * (F ** -0.5)),
        }
    _B_in = _B_setup()

    return {
        "A_x": _A_in["x"],
        "A_wqkv": _A_in["wqkv"],
        "A_wo": _A_in["wo"],
        "B_x": _B_in["x"],
        "B_w1": _B_in["w1"],
        "B_b1": _B_in["b1"],
        "B_w2": _B_in["w2"],
        "gate_w": _w((lambda *s: torch.randn(*s, dtype=torch.bfloat16) * 0.05)(1024) * 16.0),
    }

def reference(A_x, A_wqkv, A_wo, B_x, B_w1, B_b1, B_w2, gate_w):
    def _A_ref(x, wqkv, wo):
        B, S, D = x.shape
        H = 32; Dh = D // H
        qkv = (lambda x, *d: x.permute(*d))((x @ wqkv).reshape(B, S, 3, H, Dh), 2, 0, 3, 1, 4)
        q, k, v = qkv[0], qkv[1], qkv[2]
        phi = lambda t: torch.where(t > 0, t + 1, torch.exp(t)) + 1e-6
        pq, pk = phi(q), phi(k)
        kv = torch.einsum("bhsd,bhse->bhde", pk, v)
        z = torch.einsum("bhsd->bhd", pk)
        num = torch.einsum("bhqd,bhde->bhqe", pq, kv)
        den = torch.einsum("bhqd,bhd->bhq", pq, z)[..., None] + 1e-6
        o = num / den
        return (lambda x, *d: x.permute(*d))(o, 0, 2, 1, 3).reshape(B, S, D) @ wo

    def _B_ref(x, w1, b1, w2):
        h = torch.einsum("bsd,df->bsf", x, w1) + b1
        sp = torch.log1p(torch.exp(-torch.abs(h))) + torch.where(h > 0, h, torch.zeros_like(h))
        act = h * torch.tanh(sp)
        return torch.einsum("bsf,fd->bsd", act, w2)

    x = A_x
    g = _sigmoid((x * gate_w).sum(-1, keepdims=True))
    a = _A_ref(x, A_wqkv, A_wo)
    b = _B_ref(x, B_w1, B_b1, B_w2)
    return g * a + (1 - g) * b
```

```cpp
#include <hip/hip_runtime.h>
#include <hip/hip_bf16.h>
#include <stdint.h>

typedef unsigned short u16;
__device__ __forceinline__ float bf2f(u16 b) { return __uint_as_float(((unsigned)b) << 16); }
__device__ __forceinline__ u16 f2bf(float f) { unsigned u = __float_as_uint(f); u += 0x7FFFu + ((u >> 16) & 1u); return (u16)(u >> 16); }

constexpr int S = 2048, D = 1024, F = 3584, H = 32, DH = 32;

__global__ void n_gemm(const u16* A, const u16* B, float* C, int M, int N, int K, int ldc) {
    __shared__ float As[16][17], Bs[16][17];
    int tx = threadIdx.x, ty = threadIdx.y, n = blockIdx.x * 16 + tx, m = blockIdx.y * 16 + ty;
    float acc = 0.f;
    for (int k0 = 0; k0 < K; k0 += 16) {
        As[ty][tx] = bf2f(A[(size_t)m * K + k0 + tx]);
        Bs[ty][tx] = bf2f(B[(size_t)(k0 + ty) * N + n]);
        __syncthreads();
        for (int k = 0; k < 16; ++k) acc += As[ty][k] * Bs[k][tx];
        __syncthreads();
    }
    C[(size_t)m * ldc + n] = acc;
}
__global__ void n_gate(const u16* x, const u16* gw, float* g) {
    int r = blockIdx.x; float s = 0.f;
    for (int k = threadIdx.x; k < D; k += 64) s += bf2f(x[r * D + k]) * bf2f(gw[k]);
    for (int o = 32; o; o >>= 1) s += __shfl_xor(s, o);
    if (threadIdx.x == 0) g[r] = 1.f / (1.f + expf(-s));
}
__device__ float phi(float t) { return (t > 0.f ? t + 1.f : expf(t)) + 1e-6f; }
__global__ void n_kv(const float* qkv, float* kv, float* z) {
    int h = blockIdx.x, d = threadIdx.x >> 5, e = threadIdx.x & 31; float s = 0.f, zz = 0.f;
    for (int r = 0; r < S; ++r) { float pk = phi(qkv[(size_t)r * 3072 + 1024 + h * 32 + d]); s += pk * qkv[(size_t)r * 3072 + 2048 + h * 32 + e]; zz += pk; }
    kv[(h * 32 + d) * 32 + e] = s; if (e == 0) z[h * 32 + d] = zz;
}
__global__ void n_o(const float* qkv, const float* kv, const float* z, u16* o) {
    int r = blockIdx.x;
    for (int c = threadIdx.x; c < D; c += blockDim.x) { int h = c >> 5, e = c & 31; float num = 0.f, den = 0.f;
        for (int d = 0; d < 32; ++d) { float pq = phi(qkv[(size_t)r * 3072 + h * 32 + d]); num += pq * kv[(h * 32 + d) * 32 + e]; den += pq * z[h * 32 + d]; }
        o[(size_t)r * D + c] = f2bf(num / (den + 1e-6f)); }
}
__global__ void n_act(const float* h, const u16* b1, u16* act) {
    size_t i = (size_t)blockIdx.x * blockDim.x + threadIdx.x; if (i >= (size_t)S * F) return;
    float v = h[i] + bf2f(b1[i % F]); float sp = log1pf(expf(-fabsf(v))) + fmaxf(v, 0.f); act[i] = f2bf(v * tanhf(sp));
}
__global__ void n_mix(const float* a, const float* b, const float* g, u16* out) {
    size_t i = (size_t)blockIdx.x * blockDim.x + threadIdx.x; if (i >= (size_t)S * D) return;
    float gg = g[i / D]; out[i] = f2bf(gg * a[i] + (1.f - gg) * b[i]);
}

extern "C" void kernel_launch(void* const* d_in, const int* in_sizes, int n_in, void* d_out, int out_size, void* d_ws, size_t ws_size, hipStream_t stream) {
    const u16* x = (const u16*)d_in[0]; const u16* wqkv = (const u16*)d_in[1]; const u16* wo = (const u16*)d_in[2];
    const u16* w1 = (const u16*)d_in[4]; const u16* b1 = (const u16*)d_in[5]; const u16* w2 = (const u16*)d_in[6]; const u16* gw = (const u16*)d_in[7];
    char* ws = (char*)d_ws;
    float* qkv = (float*)ws; ws += (size_t)S * 3072 * 4;
    float* hh = (float*)ws; ws += (size_t)S * F * 4;
    float* aa = (float*)ws; ws += (size_t)S * D * 4;
    float* bb = (float*)ws; ws += (size_t)S * D * 4;
    float* kv = (float*)ws; ws += 32 * 1024 * 4;
    float* z = (float*)ws; ws += 1024 * 4;
    float* g = (float*)ws; ws += S * 4;
    u16* o = (u16*)ws; ws += (size_t)S * D * 2;
    u16* act = (u16*)ws; ws += (size_t)S * F * 2;
    dim3 b(16, 16);
    n_gemm<<<dim3(3072 / 16, S / 16), b, 0, stream>>>(x, wqkv, qkv, S, 3072, D, 3072);
    n_gemm<<<dim3(F / 16, S / 16), b, 0, stream>>>(x, w1, hh, S, F, D, F);
    n_gate<<<S, 64, 0, stream>>>(x, gw, g);
    n_kv<<<H, 1024, 0, stream>>>(qkv, kv, z);
    n_o<<<S, 256, 0, stream>>>(qkv, kv, z, o);
    n_act<<<(S * F + 255) / 256, 256, 0, stream>>>(hh, b1, act);
    n_gemm<<<dim3(D / 16, S / 16), b, 0, stream>>>(o, wo, aa, S, D, D, D);
    n_gemm<<<dim3(D / 16, S / 16), b, 0, stream>>>(act, w2, bb, S, D, F, D);
    n_mix<<<(S * D + 255) / 256, 256, 0, stream>>>(aa, bb, g, (u16*)d_out);
}
```

```cpp
#include <hip/hip_runtime.h>
#include <stdint.h>
#include <utility>

typedef unsigned short u16;
typedef short s16x4 __attribute__((ext_vector_type(4)));
typedef __bf16 bf16x8 __attribute__((ext_vector_type(8)));
typedef __bf16 bf16x2 __attribute__((ext_vector_type(2)));
typedef float f32x2 __attribute__((ext_vector_type(2)));
typedef float f32x4 __attribute__((ext_vector_type(4)));
typedef unsigned u32x2 __attribute__((ext_vector_type(2)));
typedef unsigned u32x4 __attribute__((ext_vector_type(4)));
typedef __attribute__((address_space(1))) unsigned gu32;
#define LAS __attribute__((address_space(3)))
#define DI __device__ __forceinline__
#define WAIT_V(n) asm volatile("s_waitcnt vmcnt(%0)" ::"n"(n) : "memory")
#define WAIT_L(n) asm volatile("s_waitcnt lgkmcnt(%0)" ::"n"(n) : "memory")
#define BAR() __builtin_amdgcn_s_barrier()
#define SCHED() __builtin_amdgcn_sched_barrier(0)
#define MFMA16(a, b, c) __builtin_amdgcn_mfma_f32_16x16x32_bf16((a), (b), (c), 0, 0, 0)

constexpr int D = 1024;
constexpr int NKG = 144;
constexpr float L2E = 1.4426950408889634f;

constexpr size_t WS_A2 = 0;
constexpr size_t WS_PQ = WS_A2 + (size_t)128 * NKG * 1024;
constexpr size_t WS_KVP = WS_PQ + (size_t)128 * 32 * 1024;
constexpr size_t WS_G = WS_KVP + (size_t)8 * 32 * 6 * 1024;
constexpr size_t WS_CNT = WS_G + 8192;
constexpr int N_CNT = 32 + 512;
constexpr size_t WS_XB = WS_CNT + N_CNT * 128;

template <int... Is> using iseq = std::integer_sequence<int, Is...>;
template <int N> using make_iseq = std::make_integer_sequence<int, N>;

DI unsigned cvt_pk(float lo, float hi) { f32x2 v = {lo, hi}; bf16x2 b = __builtin_convertvector(v, bf16x2); return __builtin_bit_cast(unsigned, b); }
DI bf16x8 pack8(const f32x4& a, const f32x4& b) { u32x4 w = {cvt_pk(a[0], a[1]), cvt_pk(a[2], a[3]), cvt_pk(b[0], b[1]), cvt_pk(b[2], b[3])}; return __builtin_bit_cast(bf16x8, w); }
DI bf16x8 cat8(const s16x4& lo, const s16x4& hi) { return __builtin_bit_cast(bf16x8, __builtin_shufflevector(lo, hi, 0, 1, 2, 3, 4, 5, 6, 7)); }
DI float phi_f(float t) { return fmaxf(t + 1.0f, __builtin_amdgcn_exp2f(-__builtin_fabsf(t * L2E))) + 1e-6f; }
DI float mish_u(float u) {
    const float e = __builtin_amdgcn_exp2f(fminf(u, 40.0f));
    const float n = e * (e + 2.0f);
    const float r = __builtin_amdgcn_rcpf(__builtin_fmaf(n, L2E, 2.0f * L2E));
    return (n * r) * u;
}
template <int CTRL> DI float dpp_add(float v) { return v + __builtin_bit_cast(float, __builtin_amdgcn_update_dpp(0, __builtin_bit_cast(int, v), CTRL, 0xf, 0xf, true)); }
DI float wave_sum(float v) {
    v = dpp_add<0xB1>(v); v = dpp_add<0x4E>(v); v = dpp_add<0x141>(v); v = dpp_add<0x140>(v);
    const int b = __builtin_bit_cast(int, v);
    return (__builtin_bit_cast(float, __builtin_amdgcn_readlane(b, 0)) + __builtin_bit_cast(float, __builtin_amdgcn_readlane(b, 16))) +
           (__builtin_bit_cast(float, __builtin_amdgcn_readlane(b, 32)) + __builtin_bit_cast(float, __builtin_amdgcn_readlane(b, 48)));
}

template <int OFF> DI void lds_b128(bf16x8& d, unsigned a) { asm volatile("ds_read_b128 %0, %1 offset:%2" : "=v"(d) : "v"(a), "i"(OFF)); }
template <int OFF> DI void lds_tr(s16x4& d, unsigned a) { asm volatile("ds_read_b64_tr_b16 %0, %1 offset:%2" : "=v"(d) : "v"(a), "i"(OFF)); }
DI void dma16(unsigned voff, const char* sbase, unsigned ldsaddr) {
    asm volatile("s_mov_b32 m0, %2\n\ts_nop 0\n\tglobal_load_lds_dwordx4 %0, %1" :: "v"(voff), "s"(sbase), "s"(ldsaddr) : "memory");
}
DI void touch(unsigned long long addr, unsigned ldsaddr) { asm volatile("s_mov_b32 m0, %1\n\ts_nop 0\n\tglobal_load_lds_dword %0, off" :: "v"(addr), "s"(ldsaddr) : "memory"); }
template <bool SC1> DI void gload16(bf16x8& d, unsigned voff, const char* sbase) {
    if constexpr (SC1) asm volatile("global_load_dwordx4 %0, %1, %2 sc1" : "=v"(d) : "v"(voff), "s"(sbase) : "memory");
    else asm volatile("global_load_dwordx4 %0, %1, %2" : "=v"(d) : "v"(voff), "s"(sbase) : "memory");
}
DI void gload16f(f32x4& d, unsigned voff, const char* sbase) { asm volatile("global_load_dwordx4 %0, %1, %2" : "=v"(d) : "v"(voff), "s"(sbase) : "memory"); }
DI void gload4(float& d, unsigned voff, const char* sbase) { asm volatile("global_load_dword %0, %1, %2" : "=v"(d) : "v"(voff), "s"(sbase) : "memory"); }
DI void gload4u_sc1(unsigned& d, unsigned voff, const char* sbase) { asm volatile("global_load_dword %0, %1, %2 sc1" : "=v"(d) : "v"(voff), "s"(sbase) : "memory"); }
DI void gstore16_sc1(const u32x4& v, unsigned voff, char* sbase) { asm volatile("global_store_dwordx4 %0, %1, %2 sc1\n\ts_nop 1" :: "v"(voff), "v"(v), "s"(sbase) : "memory"); }
DI void pstore16_sc1(char* ptr, const bf16x8& v) { const u32x4 w = __builtin_bit_cast(u32x4, v); asm volatile("global_store_dwordx4 %0, %1, off sc1\n\ts_nop 1" :: "v"(ptr), "v"(w) : "memory"); }

constexpr int K1_ASLOT = 32768, K1_BSLOT = 26624, K1_NA = 2, K1_NB = 3;
constexpr int K1_BBASE = K1_NA * K1_ASLOT;
constexpr int K1_BIAS = K1_NA * K1_ASLOT + K1_NB * K1_BSLOT;
constexpr int K1_GW = K1_BIAS + 1024;
constexpr int K1_PFD = K1_GW + 2048;
constexpr int K1_LDS = K1_PFD + 256;
struct K1P { const u16* x; const u16* wqkv; const u16* w1; const u16* b1; const u16* gw; const u16* w2; const u16* wo; char* ws; };

template <int TYPE, int GRP> struct K1C {
    static constexpr int NT = GRP == 0 ? 7 : 6, NM = NT * 4, NR = 4 + 2 * NT;
    f32x4 acc[7][4];
    bf16x8 X[2][4]; s16x4 Wl[2][7], Wh[2][7];
    unsigned va[2], vb[2], vr;
    unsigned voffA, voffQ, voffW, voffR;
    const char *sA, *sB0, *sB1, *sB2, *sR;
    unsigned dA, dB, dR;
    const char *xA, *q0, *b1p, *b2p, *rp;
    unsigned dA0, dB0, dR0, xa0, wbo0, wbo1, wro;
    int rot, T, bsf, bsn;
    unsigned long long pa; unsigned pfd; int wid;
};
constexpr int PF_T0 = 2;
template <class C> DI int k1_ktile(const C& c, int T) { return (T & ~1) | ((T + c.rot) & 1); }
template <class C> DI void k1_setA(C& c, int T, int slot) { c.sA = c.xA + k1_ktile(c, T) * 128; c.dA = c.dA0 + slot * K1_ASLOT; }
template <int TYPE, class C> DI void k1_setB(C& c, int T, int slot) { const int kt = k1_ktile(c, T);
    c.sB0 = c.q0 + (size_t)kt * (64 * 6144); c.sB1 = c.b1p + (size_t)kt * (TYPE == 0 ? 64 * 6144 : 64 * 7168); c.sB2 = c.b2p + (size_t)kt * (64 * 7168); c.sR = c.rp + (size_t)kt * (64 * 7168);
    c.dB = c.dB0 + slot * K1_BSLOT; c.dR = c.dR0 + slot * K1_BSLOT; }
template <class C> DI void k1_setRead(C& c, int aslot, int bslot) { const unsigned ao = aslot * K1_ASLOT, bo = bslot * K1_BSLOT;
    c.va[0] = c.xa0 + ao; c.va[1] = (c.xa0 ^ 64u) + ao; c.vb[0] = c.wbo0 + bo; c.vb[1] = c.wbo1 + bo; c.vr = c.wro + bo; }
template <int SET, int S_, int I, class C> DI void k1_read(C& c) {
    if constexpr (I < 4) lds_b128<I * 2048>(c.X[SET][I], c.va[S_]);
    else { constexpr int j = I - 4, tile = j >> 1, h = j & 1;
        if constexpr (tile < 6) { constexpr int off = (tile >> 1) * 8192 + S_ * 4096 + h * 512; if constexpr (h == 0) lds_tr<off>(c.Wl[SET][tile], c.vb[tile & 1]); else lds_tr<off>(c.Wh[SET][tile], c.vb[tile & 1]); }
        else { constexpr int off = S_ * 1024 + h * 256; if constexpr (h == 0) lds_tr<off>(c.Wl[SET][6], c.vr); else lds_tr<off>(c.Wh[SET][6], c.vr); } }
}
template <int TYPE, int SET, int M, class C> DI void k1_mma(C& c) {
    constexpr int tile = M >> 2, m = M & 3;
    const bf16x8 w = cat8(c.Wl[SET][tile], c.Wh[SET][tile]);
    if constexpr (TYPE == 0 && tile < 4) c.acc[tile][m] = MFMA16(c.X[SET][m], w, c.acc[tile][m]); else c.acc[tile][m] = MFMA16(w, c.X[SET][m], c.acc[tile][m]);
}
template <int TYPE, int J, class C> DI void k1_dma(C& c) {
    if constexpr (J < 4) dma16(c.voffA, c.sA + (size_t)J * 64 * 2048, c.dA + J * 8192);
    else if constexpr (J == 4) dma16(c.voffQ, c.sB0, c.dB);
    else if constexpr (J == 5) dma16(TYPE == 0 ? c.voffQ : c.voffW, c.sB1, c.dB + 8192);
    else dma16(c.voffW, c.sB2, c.dB + 16384);
}
template <class C> DI void k1_pinA(C& c) { asm volatile("" : "+s"(c.sA), "+s"(c.dA)); }
template <int GRP, class C> DI void k1_pinB(C& c) { asm volatile("" : "+s"(c.sB0), "+s"(c.sB1), "+s"(c.sB2), "+s"(c.dB)); if constexpr (GRP == 0) asm volatile("" : "+s"(c.sR), "+s"(c.dR)); }
template <int GRP, class C> DI void k1_pinR(C& c) { asm volatile("" : "+v"(c.va[0]), "+v"(c.va[1]), "+v"(c.vb[0]), "+v"(c.vb[1])); if constexpr (GRP == 0) asm volatile("" : "+v"(c.vr)); }
template <int TYPE, int CSET, int S_, int DM, bool HOOK, int M, class C> DI void k1_gap(C& c) {
    constexpr int GRP = C::NT == 7 ? 0 : 1;
    k1_mma<TYPE, CSET, M>(c);
    if constexpr (M < C::NR) k1_read<CSET ^ 1, S_, M>(c);
    if constexpr (DM == 1 && (M % 5) == 2 && M / 5 < 4) k1_dma<TYPE, M / 5>(c);
    if constexpr (DM == 2 && (M % 6) == 2 && M / 6 < 3) k1_dma<TYPE, 4 + M / 6>(c);
    if constexpr (HOOK && DM == 1 && M == 18) { if (c.T == PF_T0 + c.wid) touch(c.pa, c.pfd); }
    if constexpr (HOOK && DM == 1 && M == 20) { k1_setA(c, c.T + 3 < 16 ? c.T + 3 : 15, (c.T + 1) & 1); k1_pinA(c); }
    if constexpr (HOOK && DM == 2 && M == 17) { c.bsf = c.bsn; c.bsn = c.bsn == 2 ? 0 : c.bsn + 1; asm volatile("" : "+s"(c.bsf), "+s"(c.bsn)); }
    if constexpr (HOOK && DM == 2 && M == 18) { k1_setB<TYPE>(c, c.T + 4 < 16 ? c.T + 4 : 15, c.bsf); k1_pinB<GRP>(c); }
    if constexpr (HOOK && DM == 2 && M == 22) { k1_setRead(c, c.T & 1, c.bsn); k1_pinR<GRP>(c); }
    SCHED();
}
template <int TYPE, int CSET, int S_, int DM, bool HOOK, class C, int... Ms> DI void k1_sub_(C& c, iseq<Ms...>) { (k1_gap<TYPE, CSET, S_, DM, HOOK, Ms>(c), ...); }
template <int TYPE, int CSET, int S_, int DM, bool HOOK = false, class C> DI void k1_sub(C& c) { k1_sub_<TYPE, CSET, S_, DM, HOOK>(c, make_iseq<C::NM>{}); }
template <int SET, class C> DI void k1_fence(C& c) {
    asm volatile("s_waitcnt lgkmcnt(0)" : "+v"(c.X[SET][0]), "+v"(c.X[SET][1]), "+v"(c.X[SET][2]), "+v"(c.X[SET][3]),
                 "+v"(c.Wl[SET][0]), "+v"(c.Wh[SET][0]), "+v"(c.Wl[SET][1]), "+v"(c.Wh[SET][1]), "+v"(c.Wl[SET][2]), "+v"(c.Wh[SET][2]),
                 "+v"(c.Wl[SET][3]), "+v"(c.Wh[SET][3]), "+v"(c.Wl[SET][4]), "+v"(c.Wh[SET][4]), "+v"(c.Wl[SET][5]), "+v"(c.Wh[SET][5]) :: "memory");
    if constexpr (C::NT == 7) asm volatile("" : "+v"(c.Wl[SET][6]), "+v"(c.Wh[SET][6]));
    SCHED();
}
template <int SET, int S_, class C, int... Is> DI void k1_readall_(C& c, iseq<Is...>) { (k1_read<SET, S_, Is>(c), ...); }
template <int TYPE, int SET, class C, int... Ms> DI void k1_mmaall_(C& c, iseq<Ms...>) { (k1_mma<TYPE, SET, Ms>(c), ...); }

template <int TYPE, int GRP>
DI void k1_body(const K1P& p, LAS unsigned char* lds, const int rb, const int cb, const int wm, const int lane) {
    typedef K1C<TYPE, GRP> C;
    C c;

    const int wid = GRP * 4 + wm, pi = cb >> 1;
    const int hbase = 224 * pi, odd = pi & 1;
    const int hrem = odd ? hbase : hbase + 192, hl0 = odd ? hbase + 32 : hbase;
    c.rot = (rb + cb) & 1;
    const unsigned lbase = (unsigned)(size_t)lds;
    const int r8 = lane >> 3, c8 = lane & 7;
    c.voffA = (unsigned)((8 * wid + r8) * 2048 + ((c8 ^ (((wid & 1) << 2) | (r8 >> 1))) << 4));
    const int chB = c8 ^ ((((r8 >> 1) & 1) << 1) | ((wid & 1) << 2));
    c.voffQ = (unsigned)((8 * wid + r8) * 6144 + chB * 16); c.voffW = (unsigned)((8 * wid + r8) * 7168 + chB * 16);
    c.voffR = (unsigned)((32 * wm + 16 * (lane >> 5) + 8 * ((lane >> 3) & 1) + 4 * ((lane >> 4) & 1) + ((lane >> 1) & 3)) * 7168 + (lane & 1) * 16);
    c.xA = (const char*)p.x + (size_t)rb * 256 * 2048;
    c.q0 = (const char*)p.wqkv + (size_t)(TYPE == 0 ? 1024 + 64 * pi : 64 * pi) * 2;
    c.b1p = TYPE == 0 ? (const char*)p.wqkv + (size_t)(2048 + 64 * pi) * 2 : (const char*)p.w1 + (size_t)(hl0 + 64) * 2;
    c.b2p = (const char*)p.w1 + (size_t)(TYPE == 0 ? hl0 : hl0 + 128) * 2;
    c.rp = (const char*)p.w1 + (size_t)(hrem + 16 * TYPE) * 2;
    c.dA0 = lbase + wid * 1024; c.dB0 = lbase + K1_BBASE + wid * 1024; c.dR0 = lbase + K1_BBASE + 24576 + wm * 1024;
    auto setA = [&](int T, int slot) { k1_setA(c, T, slot); };
    auto setB = [&](int T, int slot) { k1_setB<TYPE>(c, T, slot); };
    auto setRead = [&](int aslot, int bslot) { k1_setRead(c, aslot, bslot); };
    auto issueR = [&]() { if (GRP == 0 && wm < 2) dma16(c.voffR, c.sR, c.dR); };
    auto issueA = [&]() { k1_dma<TYPE, 0>(c); k1_dma<TYPE, 1>(c); k1_dma<TYPE, 2>(c); k1_dma<TYPE, 3>(c); };
    auto issueB = [&]() { k1_dma<TYPE, 4>(c); k1_dma<TYPE, 5>(c); k1_dma<TYPE, 6>(c); };
    const int fr = lane & 15, fg = lane >> 4, fq = (lane & 15) >> 2, fp = lane & 3;
    c.xa0 = lbase + (unsigned)((64 * wm + fr) * 128 + ((fg ^ ((fr >> 1) & 7)) << 4));
    const int swB = (((fq >> 1) & 1) << 1) | ((fg & 1) << 2);
    c.wbo0 = lbase + K1_BBASE + (unsigned)(fg * 1024 + fq * 128 + (((2 * (2 * GRP) + (fp >> 1)) ^ swB) << 4) + (fp & 1) * 8);
    c.wbo1 = lbase + K1_BBASE + (unsigned)(fg * 1024 + fq * 128 + (((2 * (2 * GRP + 1) + (fp >> 1)) ^ swB) << 4) + (fp & 1) * 8);
    c.wro = lbase + K1_BBASE + (unsigned)(24576 + (fg >> 1) * 512 + (fg & 1) * 128 + fq * 32 + fp * 8);
#pragma unroll
    for (int t = 0; t < 7; ++t)
#pragma unroll
        for (int m = 0; m < 4; ++m) c.acc[t][m] = (f32x4){0.f, 0.f, 0.f, 0.f};

    setA(0, 0); issueA(); setB(0, 0); issueR(); issueB();
    if (wid == 7) dma16((unsigned)(lane < 27 ? lane : 27) * 16u, (const char*)p.b1 + hbase * 2, lbase + K1_BIAS);
    if (wid == 5) dma16((unsigned)lane * 16u, (const char*)p.gw, lbase + K1_GW);
    if (wid == 6) dma16((unsigned)lane * 16u, (const char*)p.gw + 1024, lbase + K1_GW + 1024);
    if (wid == 3) {
        unsigned long long c0, c1;
        asm volatile("s_getpc_b64 s[90:91]\n\ts_add_u32 s90, s90, gmix_k1@rel32@lo+4\n\ts_addc_u32 s91, s91, gmix_k1@rel32@hi+12\n\ts_mov_b64 %0, s[90:91]" : "=s"(c0) :: "s90", "s91", "scc");
        asm volatile("s_getpc_b64 s[90:91]\n\ts_add_u32 s90, s90, gmix_end@rel32@lo+4\n\ts_addc_u32 s91, s91, gmix_end@rel32@hi+12\n\ts_mov_b64 %0, s[90:91]" : "=s"(c1) :: "s90", "s91", "scc");
        const unsigned len = c1 > c0 && c1 - c0 < (1u << 17) ? (unsigned)(c1 - c0) : 128u;
        const unsigned per = ((len + 127u) / 128u + 31u) / 32u, off = ((blockIdx.x >> 3) * per + ((unsigned)lane < per ? (unsigned)lane : 0u)) * 128u;
        touch(c0 + (off < len ? off : 0u), lbase + K1_PFD);
    }
    setA(1, 1); issueA();
    WAIT_V(4);
    BAR(); SCHED();

    setRead(0, 0);
    k1_readall_<0, 0>(c, make_iseq<C::NR>{});
    setB(1, 1); issueR(); issueB();
    setB(2, 2); issueR();
    k1_fence<0>(c);
    k1_sub<TYPE, 0, 1, 2>(c);
    c.bsf = 0; c.bsn = 1;
    setA(2, 0); setB(3, 0); setRead(1, 1);
    { const unsigned gw_ = (unsigned)blockIdx.x * 8u + (unsigned)wid, l = lane < 36 ? (unsigned)lane : 0u;
      const unsigned long long a2 = (unsigned long long)p.w2 + (unsigned long long)(gw_ * 28u + l) * 128ull, ao = (unsigned long long)p.wo + (unsigned long long)(gw_ * 8u + ((l - 28u) & 7u)) * 128ull;
      const unsigned long long msk = l < 28u ? ~0ull : 0ull; c.pa = (a2 & msk) | (ao & ~msk);
      c.pfd = lbase + K1_PFD; c.wid = wid; }
    for (c.T = 0; c.T < 13; ++c.T) {
        if (c.T - 1 == PF_T0 + wid) WAIT_V(4); else WAIT_V(3);
        k1_fence<1>(c); BAR(); SCHED();
        issueR();
        k1_sub<TYPE, 1, 0, 1, true>(c);
        k1_fence<0>(c);
        k1_sub<TYPE, 0, 1, 2, true>(c);
    }
    const int grow = rb * 256 + cb * 8 + wid;
    u32x4 gx[2];
    {
        WAIT_V(3); k1_fence<1>(c); BAR(); SCHED();
        { const char* xr = (const char*)(p.x + (size_t)grow * D); const unsigned vo = (unsigned)lane * 16u;
          asm volatile("global_load_dwordx4 %0, %1, %2" : "=v"(gx[0]) : "v"(vo), "s"(xr) : "memory"); asm volatile("global_load_dwordx4 %0, %1, %2 offset:1024" : "=v"(gx[1]) : "v"(vo), "s"(xr) : "memory"); }
        k1_sub<TYPE, 1, 0, 1>(c);
        k1_fence<0>(c);
        k1_sub<TYPE, 0, 1, 0>(c);
    }
    {
        setRead(1, c.bsn == 2 ? 0 : c.bsn + 1);
        asm volatile("s_waitcnt vmcnt(0)" : "+v"(gx[0]), "+v"(gx[1]) :: "memory");
        k1_fence<1>(c); BAR(); SCHED();
        k1_sub<TYPE, 1, 0, 0>(c);
        { float s = 0.f;
#pragma unroll
          for (int i = 0; i < 2; ++i) { const u32x4 gg = *(LAS u32x4*)(lds + K1_GW + i * 1024 + lane * 16);
#pragma unroll
              for (int j = 0; j < 4; ++j) { s = __builtin_fmaf(__uint_as_float(gx[i][j] << 16), __uint_as_float(gg[j] << 16), s); s = __builtin_fmaf(__uint_as_float(gx[i][j] & 0xffff0000u), __uint_as_float(gg[j] & 0xffff0000u), s); } }
          s = wave_sum(s);
          if (lane == 0) ((float*)(p.ws + WS_G))[grow] = __builtin_amdgcn_rcpf(1.0f + __builtin_amdgcn_exp2f(-s * L2E)); }
        k1_fence<0>(c);
        k1_sub<TYPE, 0, 1, 0>(c);
    }
    k1_fence<1>(c);
    k1_mmaall_<TYPE, 1>(c, make_iseq<C::NM>{});

    f32x4 (&acc)[7][4] = c.acc;

    char* A2 = p.ws + WS_A2; const int rt0 = rb * 16 + wm * 4;
    auto bias4 = [&](int col0) {
        const u32x2 bw = *(LAS u32x2*)(lds + K1_BIAS + (col0 - hbase + 4 * fg) * 2);
        return (f32x4){__uint_as_float(bw.x << 16) * L2E, __uint_as_float(bw.x & 0xffff0000u) * L2E, __uint_as_float(bw.y << 16) * L2E, __uint_as_float(bw.y & 0xffff0000u) * L2E}; };
    auto act_store = [&](f32x4 (&a0)[4], f32x4 (&a1)[4], int col0) {
        const f32x4 bl0 = bias4(col0), bl1 = bias4(col0 + 16);
        char* dst = A2 + ((size_t)rt0 * NKG + (col0 >> 5)) * 1024 + lane * 16;
#pragma unroll
        for (int m = 0; m < 4; ++m) { f32x4 y0, y1;
#pragma unroll
            for (int j = 0; j < 4; ++j) { y0[j] = mish_u(__builtin_fmaf(a0[m][j], L2E, bl0[j])); y1[j] = mish_u(__builtin_fmaf(a1[m][j], L2E, bl1[j])); }
            pstore16_sc1(dst + (size_t)m * NKG * 1024, pack8(y0, y1)); }
    };
    auto rem_store = [&]() {
        const int col0 = hrem + 16 * TYPE;
        const f32x4 bl0 = bias4(col0);
        char* dst = A2 + ((size_t)rt0 * NKG + (hrem >> 5)) * 1024 + lane * 16 + TYPE * 8;
#pragma unroll
        for (int m = 0; m < 4; ++m) { f32x4 y;
#pragma unroll
            for (int j = 0; j < 4; ++j) y[j] = mish_u(__builtin_fmaf(acc[6][m][j], L2E, bl0[j]));
            *(u32x2*)(dst + (size_t)m * NKG * 1024) = (u32x2){cvt_pk(y[0], y[1]), cvt_pk(y[2], y[3])}; }
    };
    const int head = 2 * pi + GRP;
    if (TYPE == 1) {
        char* PQ = p.ws + WS_PQ;
#pragma unroll
        for (int m = 0; m < 4; ++m) pstore16_sc1(PQ + ((size_t)(rt0 + m) * 32 + head) * 1024 + lane * 16, pack8(acc[0][m], acc[1][m]));
        act_store(acc[2], acc[3], hl0 + 64 + 32 * GRP);
        act_store(acc[4], acc[5], hl0 + 128 + 32 * GRP);
        if (GRP == 0) rem_store();
    } else {
        f32x4 kv[3][2];
#pragma unroll
        for (int t = 0; t < 3; ++t) { kv[t][0] = (f32x4){0.f, 0.f, 0.f, 0.f}; kv[t][1] = (f32x4){0.f, 0.f, 0.f, 0.f}; }
        const u32x4 onesw = {0x3f803f80u, 0x3f803f80u, 0x3f803f80u, 0x3f803f80u}; const bf16x8 ones = __builtin_bit_cast(bf16x8, onesw);
#pragma unroll
        for (int mp = 0; mp < 2; ++mp) { bf16x8 pk[2], vf[2];
#pragma unroll
            for (int t = 0; t < 2; ++t) { f32x4 y0, y1;
#pragma unroll
                for (int j = 0; j < 4; ++j) { y0[j] = phi_f(acc[t][2 * mp][j]); y1[j] = phi_f(acc[t][2 * mp + 1][j]); }
                pk[t] = pack8(y0, y1); vf[t] = pack8(acc[2 + t][2 * mp], acc[2 + t][2 * mp + 1]); }
#pragma unroll
            for (int dt = 0; dt < 2; ++dt) { kv[0][dt] = MFMA16(pk[dt], vf[0], kv[0][dt]); kv[1][dt] = MFMA16(pk[dt], vf[1], kv[1][dt]); kv[2][dt] = MFMA16(pk[dt], ones, kv[2][dt]); } }
        LAS f32x4* red = (LAS f32x4*)(lds + K1_BBASE + K1_BSLOT);
#pragma unroll
        for (int t = 0; t < 3; ++t)
#pragma unroll
            for (int dt = 0; dt < 2; ++dt) red[(wid * 6 + t * 2 + dt) * 64 + lane] = kv[t][dt];
        act_store(acc[4], acc[5], hl0 + 32 * GRP);
        if (GRP == 0) rem_store();
        WAIT_L(0); BAR(); SCHED();
        f32x4* KVP = (f32x4*)(p.ws + WS_KVP);
        for (int ci = wid; ci < 12; ci += 8) { const int g2 = ci / 6, cc = ci % 6;
            const f32x4 s4 = (red[((g2 * 4 + 0) * 6 + cc) * 64 + lane] + red[((g2 * 4 + 1) * 6 + cc) * 64 + lane]) + (red[((g2 * 4 + 2) * 6 + cc) * 64 + lane] + red[((g2 * 4 + 3) * 6 + cc) * 64 + lane]);
            KVP[(((size_t)rb * 32 + 2 * pi + g2) * 6 + cc) * 64 + lane] = s4; }
    }

}

extern "C" __global__ __launch_bounds__(512, 2) void gmix_k1(K1P p) {
    extern __shared__ __attribute__((aligned(16))) unsigned char shm[];
    LAS unsigned char* lds = (LAS unsigned char*)shm;
    const int b = blockIdx.x, xcd = b & 7, idx = b >> 3;
    const int rb = (xcd >> 2) * 4 + (idx >> 3), cb = (xcd & 3) * 8 + (idx & 7);
    const int wid = __builtin_amdgcn_readfirstlane(threadIdx.x >> 6), lane = threadIdx.x & 63, wm = wid & 3;
    if (threadIdx.x < 3 && b * 3 + (int)threadIdx.x < N_CNT) *(u32x4*)(p.ws + WS_CNT + (b * 3 + threadIdx.x) * 128) = (u32x4){0u, 0u, 0u, 0u};
    if (cb & 1) { if (wid >= 4) k1_body<1, 1>(p, lds, rb, cb, wm, lane); else k1_body<1, 0>(p, lds, rb, cb, wm, lane); }
    else        { if (wid >= 4) k1_body<0, 1>(p, lds, rb, cb, wm, lane); else k1_body<0, 0>(p, lds, rb, cb, wm, lane); }
}

constexpr int K2_NA = 28, K2_NJ = 44, K2_PA = 6, K2_NSLOT = 16, K2_PER = 6, K2_LBS = 10, K2_LXS = 4;
constexpr int K2_KVL = K2_NSLOT * 8192;
constexpr int K2_LDS = K2_KVL + 3072;
struct K2P { const u16* w2; const u16* wo; char* ws; u16* out; };

constexpr int k2_grpB(int k) { return k < 4 ? -1 : (k - K2_LBS > (k - 4) / 2 ? k - K2_LBS : (k - 4) / 2); }
constexpr int k2_grpX(int k) { return k < 2 ? -1 : (k - K2_LXS > (k - 2) / 2 ? k - K2_LXS : (k - 2) / 2); }
constexpr bool k2_isbar(int j) { return j == 3 || (j > 0 && j % K2_PER == 0); }
constexpr int k2_nextbar(int j) { int g = j + 1; while (g < K2_NJ && !k2_isbar(g)) ++g; return g; }
constexpr int K2_G_P0 = 5, K2_N_P0 = 12;
constexpr int K2_G_P1 = 10;
constexpr int K2_G_ST = 12, K2_N_ST = 2;
constexpr int K2_G_SIG = 18;
constexpr int K2_G_FL = 20;
constexpr int K2_G_XS = K2_NA;
constexpr int K2_G_XF = 35;
constexpr int K2_G_XP = 38;
constexpr int K2_G_XL = 40, K2_N_XL = 6;
constexpr int k2_extras(int g) { return (g == K2_G_P0 ? K2_N_P0 : 0) + (g == K2_G_ST ? K2_N_ST : 0) + (g == K2_G_FL ? 1 : 0) + (g >= K2_G_XS && g < K2_G_XS + 3 ? 2 : 0) + (g == K2_G_XF ? 1 : 0) + (g == K2_G_XP ? 1 : 0) + (g == K2_G_XL ? K2_N_XL : 0); }
constexpr int k2_nops(int g) { int n = 0; for (int k = 0; k < K2_NJ; ++k) { n += k2_grpB(k) == g; n += 2 * (k2_grpX(k) == g); } return n + k2_extras(g); }
constexpr int k2_span(int g0, int g1) { int n = 0; for (int h = g0; h <= g1; ++h) n += k2_nops(h); return n; }
constexpr int k2_since(int g0, int g1) { return k2_span(g0 + 1, g1) - k2_extras(g1); }
constexpr int k2_youngX(int k, int j) { const int g = k2_grpX(k); int n = k2_extras(g); for (int q = k + 1; q < K2_NJ; ++q) n += 2 * (k2_grpX(q) == g); if (g == -1) for (int q = 0; q < K2_NJ; ++q) n += (k2_grpB(q) == -1 && q > 2 * k + 1);
    return n + k2_span(g + 1, j - 1); }
constexpr int k2_youngB(int k, int j) { const int g = k2_grpB(k); int n = k2_extras(g);
    if (g == -1) { for (int q = k + 1; q < 4; ++q) n += 1; for (int q = 0; q < 2; ++q) n += 2 * (2 * q + 1 >= k); }
    else { for (int q = k + 1; q < K2_NJ; ++q) n += k2_grpB(q) == g; for (int q = 0; q < K2_NJ; ++q) n += 2 * (k2_grpX(q) == g); }
    return n + k2_span(g + 1, j - 1); }
constexpr int k2_min(int a, int b) { return a < b ? a : b; }
constexpr int k2_wait(int j) {
    int n = k2_youngX(j, j);
    if (k2_isbar(j)) { const int kb = k2_min(k2_nextbar(j), K2_NJ - 1); n = k2_min(n, k2_youngB(kb, j)); }
    if (j == K2_G_SIG) n = k2_min(n, k2_span(K2_G_ST + 1, j - 1));
    if (j == K2_G_XF) n = k2_min(n, k2_span(K2_G_XS + 3, j - 1));
    return n > 63 ? 63 : n;
}

struct K2C {
    f32x4 acc[2][8];
    bf16x8 X[K2_PA][2];
    s16x4 Wl[2][8], Wh[2][8];
    unsigned wb[2][4];
    unsigned voffB, voffBo, voffX0, voffX1;
    const char *w2b, *wob, *a2b, *a2o;
    unsigned dB;
    unsigned rot;
    u32x4 pq[2];
    u32x4 xs[6]; char* xd[3];
};
template <int J> DI void k2_dmaB(K2C& c) {
    if constexpr (J < K2_NA) dma16(c.voffB, c.w2b + (size_t)(J & ~1) * 65536 + (((J & 1) ^ c.rot) << 16), c.dB + (J % K2_NSLOT) * 8192);
    else dma16(c.voffBo, c.wob + (size_t)((J - K2_NA) & ~1) * 131072 + (((J & 1) ^ c.rot) << 17), c.dB + (J % K2_NSLOT) * 8192);
}
template <int J> DI void k2_ldX(K2C& c) { constexpr int SLOT = J % K2_PA;
    if constexpr (J < K2_NA) { const char* src = c.a2b + (size_t)(J & ~1) * 1024 + (((J & 1) ^ c.rot) << 10);
        gload16<true>(c.X[SLOT][0], c.voffX0, src); gload16<true>(c.X[SLOT][1], c.voffX1, src); }
    else { const char* src = c.a2o + (size_t)((J - K2_NA) & ~1) * 2048 + (((J & 1) ^ c.rot) << 11);
        gload16<true>(c.X[SLOT][0], c.voffX0, src); gload16<true>(c.X[SLOT][1], c.voffX0, src + 1024); }
}
template <int SET, int J, int I> DI void k2_read(K2C& c) {
    constexpr int SLOT = J % K2_NSLOT, t = I >> 1, h = I & 1;
    constexpr int off = (SLOT % 8) * 8192 + (J < K2_NA ? (t >> 2) * 1024 + h * 4096 : (t >> 2) * 4096 + h * 2048);
    if constexpr (h == 0) lds_tr<off>(c.Wl[SET][t], c.wb[SLOT / 8][t & 3]); else lds_tr<off>(c.Wh[SET][t], c.wb[SLOT / 8][t & 3]);
}
template <int SET, int J, int... Is> DI void k2_readall(K2C& c, iseq<Is...>) { (k2_read<SET, J, Is>(c), ...); }
constexpr int k2_nthB(int g, int n) { for (int k = 0; k < K2_NJ; ++k) if (k2_grpB(k) == g && n-- == 0) return k; return -1; }
constexpr int k2_nthX(int g, int n) { for (int k = 0; k < K2_NJ; ++k) if (k2_grpX(k) == g && n-- == 0) return k; return -1; }
template <int J, int M> DI void k2_gap(K2C& c) {
    constexpr int SET = J & 1, XS = J % K2_PA;
    if constexpr (J < K2_NA) {
        constexpr int t = M >> 1, m = M & 1;
        c.acc[m][t] = MFMA16(cat8(c.Wl[SET][t], c.Wh[SET][t]), c.X[XS][m], c.acc[m][t]);
        if constexpr (M < 8) { k2_read<SET ^ 1, J + 1, 2 * M>(c); k2_read<SET ^ 1, J + 1, 2 * M + 1>(c); }
        if constexpr (J == K2_G_P1 + 1 && (M & 1) == 1) { constexpr int d = M >> 1; const unsigned w = c.pq[d >> 2][d & 3];
            c.pq[d >> 2][d & 3] = cvt_pk(phi_f(__uint_as_float(w << 16)), phi_f(__uint_as_float(w & 0xffff0000u))); asm volatile("" : "+v"(c.pq[d >> 2][d & 3])); }
        if constexpr (M == 8 && k2_nthB(J, 0) >= 0) k2_dmaB<k2_nthB(J, 0)>(c);
        if constexpr (M == 9 && k2_nthB(J, 1) >= 0) k2_dmaB<k2_nthB(J, 1)>(c);
        if constexpr (M == 11 && k2_nthX(J, 0) >= 0) k2_ldX<k2_nthX(J, 0)>(c);
        if constexpr (M == 13 && k2_nthX(J, 1) >= 0) k2_ldX<k2_nthX(J, 1)>(c);
    } else {
        c.acc[0][M & 3] = MFMA16(cat8(c.Wl[SET][M], c.Wh[SET][M]), c.X[XS][M >> 2], c.acc[0][M & 3]);
        if constexpr (J + 1 < K2_NJ && M < 4) { k2_read<SET ^ 1, J + 1, 4 * M>(c); k2_read<SET ^ 1, J + 1, 4 * M + 1>(c); k2_read<SET ^ 1, J + 1, 4 * M + 2>(c); k2_read<SET ^ 1, J + 1, 4 * M + 3>(c); }
        if constexpr (M == 4 && k2_nthB(J, 0) >= 0) k2_dmaB<k2_nthB(J, 0)>(c);
        if constexpr (M == 5 && k2_nthX(J, 0) >= 0) k2_ldX<k2_nthX(J, 0)>(c);
        if constexpr (J >= K2_G_XS && J < K2_G_XS + 3 && M >= 6) gstore16_sc1(c.xs[2 * (J - K2_G_XS) + (M - 6)], c.voffX0, c.xd[J - K2_G_XS] + (M - 6) * 1024);
    }
    SCHED();
}
template <int J, int... Ms> DI void k2_gaps(K2C& c, iseq<Ms...>) { (k2_gap<J, Ms>(c), ...); }
template <int J> DI void k2_group(K2C& c) {
    constexpr int SET = J & 1;
    asm volatile("s_waitcnt vmcnt(%18) lgkmcnt(0)" : "+v"(c.X[J % K2_PA][0]), "+v"(c.X[J % K2_PA][1]),
                 "+v"(c.Wl[SET][0]), "+v"(c.Wh[SET][0]), "+v"(c.Wl[SET][1]), "+v"(c.Wh[SET][1]), "+v"(c.Wl[SET][2]), "+v"(c.Wh[SET][2]), "+v"(c.Wl[SET][3]), "+v"(c.Wh[SET][3]),
                 "+v"(c.Wl[SET][4]), "+v"(c.Wh[SET][4]), "+v"(c.Wl[SET][5]), "+v"(c.Wh[SET][5]), "+v"(c.Wl[SET][6]), "+v"(c.Wh[SET][6]), "+v"(c.Wl[SET][7]), "+v"(c.Wh[SET][7]) : "n"(k2_wait(J)) : "memory");
    if constexpr (k2_isbar(J)) BAR();
    SCHED();
    k2_gaps<J>(c, make_iseq<(J < K2_NA ? 16 : 8)>{});
}
template <int J0, int... Is> DI void k2_groups_(K2C& c, iseq<Is...>) { (k2_group<J0 + Is>(c), ...); }
template <int J0, int J1> DI void k2_groups(K2C& c) { k2_groups_<J0>(c, make_iseq<J1 - J0 + 1>{}); }
constexpr bool k2_check() { for (int k = K2_NSLOT; k < K2_NJ; ++k) { const int g = k2_grpB(k); bool ok = false; for (int h = k - K2_NSLOT; h <= g; ++h) ok = ok || k2_isbar(h); if (!ok) return false; }
    for (int k = K2_PA; k < K2_NJ; ++k) if (k2_grpX(k) <= k - K2_PA) return false;
    for (int k = 0; k < K2_NJ; ++k) { if (k2_nthB(k, 2) >= 0 || k2_nthX(k, 2) >= 0) return false; if (k >= K2_NA && (k2_nthB(k, 1) >= 0 || k2_nthX(k, 1) >= 0)) return false; }
    for (int k = 4; k < K2_NJ; ++k) { int bg = k - 1; while (bg > 0 && !k2_isbar(bg)) --bg; if (bg == 0 || k2_grpB(k) >= bg) return false; }
    return k2_isbar(K2_G_ST) && k2_isbar(K2_G_SIG) && K2_G_FL < k2_grpX(K2_NA); }
static_assert(k2_check(), "K2 schedule");

extern "C" __global__ __launch_bounds__(512, 2) void gmix_k2(K2P p) {
    extern __shared__ __attribute__((aligned(16))) unsigned char shm[];
    LAS unsigned char* lds = (LAS unsigned char*)shm;
    const unsigned lbase = (unsigned)(size_t)lds;
    const int b = blockIdx.x, xcd = b & 7, idx = b >> 3;
    const int ks = xcd & 3, rb = (xcd >> 2) * 4 + (idx >> 3), cb = idx & 7;
    const int wid = __builtin_amdgcn_readfirstlane(threadIdx.x >> 6), lane = threadIdx.x & 63;
    const int r8 = lane >> 3, c8 = lane & 7, fr = lane & 15, fg = lane >> 4, fq = (lane & 15) >> 2, fp = lane & 3;
    K2C c;
    c.rot = (unsigned)(rb + cb) & 1u;
    c.voffB = (unsigned)((8 * (wid >> 1) + r8) * 2048 + (wid & 1) * 128 + ((c8 ^ (r8 & 6)) << 4));
    c.voffBo = (unsigned)((8 * wid + r8) * 2048 + ((c8 ^ (r8 & 6)) << 4));
    c.w2b = (const char*)p.w2 + cb * 256 + (size_t)ks * (K2_NA * 65536); c.wob = (const char*)p.wo + cb * 256 + (ks & 1) * 128;
    const int rt0 = rb * 16 + wid * 2;
    c.a2b = p.ws + WS_A2 + ((size_t)rt0 * NKG + K2_NA * ks) * 1024; c.a2o = p.ws + WS_A2 + ((size_t)(rt0 + (ks >> 1)) * NKG + 112) * 1024;
    c.voffX0 = (unsigned)lane * 16u; c.voffX1 = c.voffX0 + NKG * 1024;
    c.dB = lbase + wid * 1024;
    const int head = cb * 4 + ks;
    k2_dmaB<0>(c); k2_dmaB<1>(c); k2_ldX<0>(c); k2_dmaB<2>(c); k2_dmaB<3>(c); k2_ldX<1>(c);
    const int rr = (4 * fg + fq) & 7;
#pragma unroll
    for (int t4 = 0; t4 < 4; ++t4) { c.wb[0][t4] = lbase + (unsigned)((fg >> 1) * 2048 + rr * 128 + (((2 * t4 + (fp >> 1)) ^ (rr & 6)) << 4) + (fp & 1) * 8); c.wb[1][t4] = c.wb[0][t4] + 8 * 8192; }
#pragma unroll
    for (int m = 0; m < 2; ++m)
#pragma unroll
        for (int t = 0; t < 8; ++t) c.acc[m][t] = (f32x4){0.f, 0.f, 0.f, 0.f};
    WAIT_V(2); BAR(); SCHED();
    k2_readall<0, 0>(c, make_iseq<16>{});
    gu32* cnt = (gu32*)(p.ws + WS_CNT);
    k2_groups<0, K2_G_P0>(c);
    f32x4 kvp[8]; bf16x8 pq[2]; float g[2];
    { const int comp = wid < 6 ? wid : wid - 2; const unsigned vo = wid < 4 ? c.voffX0 : (unsigned)fg * 256u;
#pragma unroll
      for (int r = 0; r < 8; ++r) gload16f(kvp[r], vo, p.ws + WS_KVP + (((size_t)r * 32 + head) * 6 + comp) * 1024); }
    gload16<false>(pq[0], c.voffX0, p.ws + WS_PQ + ((size_t)rt0 * 32 + head) * 1024);
    gload16<false>(pq[1], c.voffX0, p.ws + WS_PQ + ((size_t)(rt0 + 1) * 32 + head) * 1024);
    gload4(g[0], (unsigned)fr * 4u, p.ws + WS_G + (size_t)rt0 * 64); gload4(g[1], (unsigned)fr * 4u, p.ws + WS_G + (size_t)(rt0 + 1) * 64);
    SCHED();
    k2_groups<K2_G_P0 + 1, K2_G_P1>(c);
    asm volatile("s_waitcnt vmcnt(%12)" : "+v"(kvp[0]), "+v"(kvp[1]), "+v"(kvp[2]), "+v"(kvp[3]), "+v"(kvp[4]), "+v"(kvp[5]), "+v"(kvp[6]), "+v"(kvp[7]), "+v"(pq[0]), "+v"(pq[1]), "+v"(g[0]), "+v"(g[1])
                 : "n"(k2_since(K2_G_P0, K2_G_P1)) : "memory");
    c.pq[0] = __builtin_bit_cast(u32x4, pq[0]); c.pq[1] = __builtin_bit_cast(u32x4, pq[1]);
    if (wid < 6) { const f32x4 sm = ((kvp[0] + kvp[1]) + (kvp[2] + kvp[3])) + ((kvp[4] + kvp[5]) + (kvp[6] + kvp[7]));
        const u32x2 w = {cvt_pk(sm[0], sm[1]), cvt_pk(sm[2], sm[3])};
        asm volatile("ds_write_b64 %0, %1" :: "v"(lbase + K2_KVL + (wid >> 1) * 1024 + lane * 16 + (wid & 1) * 8), "v"(w) : "memory"); }
    WAIT_L(0);
    const float g1a = 1.0f - g[0], g1b = 1.0f - g[1];
    SCHED();
    k2_groups<K2_G_P1 + 1, K2_G_ST>(c);
    {
        bf16x8 k0, k1, zf; const unsigned ka = lbase + K2_KVL + lane * 16;
        lds_b128<0>(k0, ka); lds_b128<1024>(k1, ka); lds_b128<2048>(zf, ka);
        asm volatile("s_waitcnt lgkmcnt(0)" : "+v"(k0), "+v"(k1), "+v"(zf) :: "memory");
        SCHED();
#pragma unroll
        for (int i = 0; i < 2; ++i) { const f32x4 z4 = {0.f, 0.f, 0.f, 0.f};
            const bf16x8 pf_ = __builtin_bit_cast(bf16x8, c.pq[i]);
            const f32x4 n0 = MFMA16(k0, pf_, z4), n1 = MFMA16(k1, pf_, z4), dn = MFMA16(zf, pf_, z4);
            const float sc = g[i] * __builtin_amdgcn_rcpf(dn[0] + 1e-6f);
            gstore16_sc1(__builtin_bit_cast(u32x4, pack8(n0 * sc, n1 * sc)), c.voffX0, p.ws + WS_A2 + ((size_t)(rt0 + i) * NKG + 112 + head) * 1024); }
        SCHED();
    }
    k2_groups<K2_G_ST + 1, K2_G_SIG>(c);
    if (threadIdx.x == 0) __hip_atomic_fetch_add(cnt + (rb * 4 + (head >> 3)) * 32, 1u, __ATOMIC_RELAXED, __HIP_MEMORY_SCOPE_AGENT);
    k2_groups<K2_G_SIG + 1, K2_G_FL>(c);
    unsigned flag; const unsigned cvo = (unsigned)(lane & 3) * 128u;
    gload4u_sc1(flag, cvo, (const char*)(cnt + rb * 4 * 32));
    constexpr int GO = k2_grpX(K2_NA);
    k2_groups<K2_G_FL + 1, GO - 1>(c);
    asm volatile("s_waitcnt vmcnt(%1)" : "+v"(flag) : "n"(k2_since(K2_G_FL, GO - 1)) : "memory");
    if (__builtin_amdgcn_ballot_w64(flag < 8u) != 0ull) { unsigned spins = 0;
        for (;;) { const unsigned f = __hip_atomic_load(cnt + (rb * 4 + (lane & 3)) * 32, __ATOMIC_RELAXED, __HIP_MEMORY_SCOPE_AGENT);
            if (__builtin_amdgcn_ballot_w64(f < 8u) == 0ull || ++spins > (1u << 22)) break; __builtin_amdgcn_s_sleep(2); } }
    SCHED();
    k2_groups<GO, K2_NA - 2>(c);
#pragma unroll
    for (int t4 = 0; t4 < 4; ++t4) { c.wb[0][t4] -= (unsigned)(fg >> 1) * 1024u; c.wb[1][t4] -= (unsigned)(fg >> 1) * 1024u; }
    k2_groups<K2_NA - 1, K2_NA - 1>(c);
    const int tile = rb * 8 + cb;
    char* xb = p.ws + WS_XB + (size_t)tile * (12 * 8 * 2048);
#pragma unroll
    for (int t = 0; t < 8; ++t) { c.acc[0][t] *= g1a; c.acc[1][t] *= g1b; }
#pragma unroll
    for (int i = 0; i < 3; ++i) c.xd[i] = xb + (size_t)((((ks + 1 + i) & 3) * 3 + (2 - i)) * 8 + wid) * 2048;
#define K2_PACK(i, m, t0) do { c.xs[2 * (i)] = __builtin_bit_cast(u32x4, pack8(c.acc[m][t0], c.acc[m][(t0) + 1])); c.xs[2 * (i) + 1] = __builtin_bit_cast(u32x4, pack8(c.acc[m][(t0) + 2], c.acc[m][(t0) + 3])); } while (0)
#define K2_KEEP(m, t0) do { const f32x4 k0_ = c.acc[m][t0], k1_ = c.acc[m][(t0) + 1], k2_ = c.acc[m][(t0) + 2], k3_ = c.acc[m][(t0) + 3]; c.acc[0][0] = k0_; c.acc[0][1] = k1_; c.acc[0][2] = k2_; c.acc[0][3] = k3_; } while (0)
    if (ks == 0) { K2_PACK(0, 0, 4); K2_PACK(1, 1, 0); K2_PACK(2, 1, 4); }
    else if (ks == 1) { K2_PACK(0, 1, 0); K2_PACK(1, 1, 4); K2_PACK(2, 0, 0); K2_KEEP(0, 4); }
    else if (ks == 2) { K2_PACK(0, 1, 4); K2_PACK(1, 0, 0); K2_PACK(2, 0, 4); K2_KEEP(1, 0); }
    else { K2_PACK(0, 0, 0); K2_PACK(1, 0, 4); K2_PACK(2, 1, 0); K2_KEEP(1, 4); }
#undef K2_PACK
#undef K2_KEEP
    SCHED();
    gu32* xc = cnt + (32 + tile * 8 + wid) * 32;
    k2_groups<K2_NA, K2_G_XF>(c);
    if (lane == 0) __hip_atomic_store(xc + ks, 1u, __ATOMIC_RELAXED, __HIP_MEMORY_SCOPE_AGENT);
    k2_groups<K2_G_XF + 1, K2_G_XP>(c);
    gload4u_sc1(flag, (unsigned)(lane & 3) * 4u, (const char*)xc);
    k2_groups<K2_G_XP + 1, K2_G_XL>(c);
    asm volatile("s_waitcnt vmcnt(%1)" : "+v"(flag) : "n"(k2_since(K2_G_XP, K2_G_XL)) : "memory");
    if (__builtin_amdgcn_ballot_w64(flag != 1u) != 0ull) { unsigned spins = 0;
        for (;;) { const unsigned f = __hip_atomic_load(xc + (lane & 3), __ATOMIC_RELAXED, __HIP_MEMORY_SCOPE_AGENT);
            if (__builtin_amdgcn_ballot_w64(f != 1u) == 0ull || ++spins > (1u << 22)) break; __builtin_amdgcn_s_sleep(1); } }
    u32x4 r[3][2];
    { const char* sp = xb + (size_t)(ks * 3 * 8 + wid) * 2048;
#pragma unroll
      for (int i = 0; i < 3; ++i)
#pragma unroll
          for (int h = 0; h < 2; ++h) asm volatile("global_load_dwordx4 %0, %1, %2 offset:%3 sc1" : "=v"(r[i][h]) : "v"(c.voffX0), "s"(sp + (size_t)i * 8 * 2048), "i"(h * 1024) : "memory"); }
    SCHED();
    k2_groups<K2_G_XL + 1, K2_NJ - 1>(c);
    asm volatile("s_waitcnt vmcnt(0)" : "+v"(r[0][0]), "+v"(r[0][1]), "+v"(r[1][0]), "+v"(r[1][1]), "+v"(r[2][0]), "+v"(r[2][1]) :: "memory");
    f32x4 o[4] = {c.acc[0][0], c.acc[0][1], c.acc[0][2], c.acc[0][3]};
#pragma unroll
    for (int i = 0; i < 3; ++i)
#pragma unroll
        for (int h = 0; h < 2; ++h) { const u32x4 w = r[i][h];
            o[2 * h] += (f32x4){__uint_as_float(w[0] << 16), __uint_as_float(w[0] & 0xffff0000u), __uint_as_float(w[1] << 16), __uint_as_float(w[1] & 0xffff0000u)};
            o[2 * h + 1] += (f32x4){__uint_as_float(w[2] << 16), __uint_as_float(w[2] & 0xffff0000u), __uint_as_float(w[3] << 16), __uint_as_float(w[3] & 0xffff0000u)}; }
    u16* orow = p.out + (size_t)(rb * 256 + wid * 32 + (ks >> 1) * 16 + fr) * D + cb * 128 + (ks & 1) * 64 + ((fg & 1) * 16 + (fg >> 1) * 8);
#pragma unroll
    for (int h = 0; h < 2; ++h) {
        const unsigned a0 = cvt_pk(o[2 * h][0], o[2 * h][1]), a1 = cvt_pk(o[2 * h][2], o[2 * h][3]), b0 = cvt_pk(o[2 * h + 1][0], o[2 * h + 1][1]), b1 = cvt_pk(o[2 * h + 1][2], o[2 * h + 1][3]);
        const auto s0 = __builtin_amdgcn_permlane16_swap(a0, b0, false, false), s1 = __builtin_amdgcn_permlane16_swap(a1, b1, false, false);
        *(u32x4*)(orow + h * 32) = (u32x4){s0[0], s1[0], s0[1], s1[1]};
    }
}

extern "C" __global__ void gmix_end() {}

extern "C" void kernel_launch(void* const* d_in, const int* in_sizes, int n_in, void* d_out, int out_size, void* d_ws, size_t ws_size, hipStream_t stream) {
    (void)in_sizes; (void)n_in; (void)out_size; (void)ws_size;
    (void)hipFuncSetAttribute((const void*)gmix_k1, hipFuncAttributeMaxDynamicSharedMemorySize, K1_LDS);
    (void)hipFuncSetAttribute((const void*)gmix_k2, hipFuncAttributeMaxDynamicSharedMemorySize, K2_LDS);
    K1P p1{(const u16*)d_in[0], (const u16*)d_in[1], (const u16*)d_in[4], (const u16*)d_in[5], (const u16*)d_in[7], (const u16*)d_in[6], (const u16*)d_in[2], (char*)d_ws};
    gmix_k1<<<256, 512, K1_LDS, stream>>>(p1);
    K2P p2{(const u16*)d_in[6], (const u16*)d_in[2], (char*)d_ws, (u16*)d_out};
    gmix_k2<<<256, 512, K2_LDS, stream>>>(p2);
}
```
